```python
import torch

def _softmax(x, axis=-1):
    return torch.softmax(x, dim=axis)
def _gelu(x):
    return torch.nn.functional.gelu(x, approximate="tanh")
def _silu(x):
    return torch.nn.functional.silu(x)
def _sigmoid(x):
    return torch.sigmoid(x)
def _layernorm(x, axis=-1, eps=1e-5):
    m = x.mean(dim=axis, keepdim=True)
    v = x.var(dim=axis, keepdim=True, unbiased=False)
    return (x - m) / torch.sqrt(v + eps)
def _scatter_add(tgt, idx, src, axis=0):
    return tgt.clone().index_add_(axis, idx, src)
def _segment_sum(src, seg, n):
    out = torch.zeros((n,) + src.shape[1:], dtype=src.dtype)
    return out.index_add_(0, seg, src)
def _pad(x, pw):
    flat = tuple(v for pair in reversed(pw) for v in pair)
    return torch.nn.functional.pad(x, flat)

kernel_name = 'standalone__weight_only_int_squeezellm__prompt'

def setup_inputs():

    _w = (lambda a: (lambda t: t.to(torch.bfloat16) if t.is_floating_point() else t)(torch.as_tensor(a)))
    B, S, D, F = 1, 2048, 1024, 3584
    bits = 3 if (2 <= 3 <= 4) else 4
    L = 2 ** bits
    qw = torch.randint(0, L, (D, F)).to(torch.int64)
    lut = (lambda *s: torch.randn(*s, dtype=torch.bfloat16) * 0.05)(L, F)
    return {
        "x": _w((lambda *s: torch.randn(*s, dtype=torch.bfloat16) * 0.05)(B, S, D)),
        "qw": _w(qw),
        "lut": _w(lut),
    }

def reference(x, qw, lut):
    w = (lambda x, i, axis: torch.take_along_dim(x, i, dim=axis))(lut, qw, axis=0)
    w = (lambda x, dt: x.to(dt))(w, x.dtype)
    return torch.einsum('bsd,df->bsf', x, w)
```

```cpp
#include <hip/hip_runtime.h>
#include <hip/hip_bf16.h>
using bf16=__hip_bfloat16;
using bf16x8=__attribute__((ext_vector_type(8)))short;
using f32x4=__attribute__((ext_vector_type(4)))float;
constexpr int M_=2048,K_=1024,N_=3584;

__global__ void dequant_kernel(const int* __restrict__ qw,const unsigned short* __restrict__ lut,unsigned short* __restrict__ wt){
  __shared__ unsigned short t[64][66];
  int n0=blockIdx.x*64,k0=blockIdx.y*64;
  int tx=threadIdx.x&63,ty=threadIdx.x>>6;
  for(int r=ty;r<64;r+=4){
    int q=qw[(long)(k0+r)*N_+n0+tx];
    t[tx][r]=lut[q*N_+n0+tx];
  }
  __syncthreads();
  for(int r=ty;r<64;r+=4){
    wt[(long)(n0+r)*K_+k0+tx]=t[r][tx];
  }
}

constexpr int BM=128,BK=32,HT=BM*BK;
__global__ __launch_bounds__(256,2)
void gemm_128sq(const bf16*A,const bf16*Bt,bf16*C){
  __shared__ __attribute__((aligned(16))) bf16 shm[2*HT];
  const int M=M_,N=N_,K=K_;
  int blk=blockIdx.x;
  int brow=(blk/(N/BM))*BM,bcol=(blk%(N/BM))*BM;
  int wid=threadIdx.x>>6,lane=threadIdx.x&63,wr=wid>>1,wc=wid&1,fr=lane&15,fq=lane>>4;
  bf16*SA=shm,*SB=shm+HT;
  auto stage=[&](int kt){
    for(int i=0;i<2;++i){int b=threadIdx.x*16+i*4096,r=b/64,c=(b%64)/2;
      __builtin_amdgcn_global_load_lds((const unsigned*)(A+(long)(brow+r)*K+kt*BK+c),(unsigned*)((char*)SA+b),16,0,0);
      __builtin_amdgcn_global_load_lds((const unsigned*)(Bt+(long)(bcol+r)*K+kt*BK+c),(unsigned*)((char*)SB+b),16,0,0);}};
  auto ld=[&](bf16x8 d[4],bf16*base,int off){for(int m=0;m<4;++m)
    d[m]=*reinterpret_cast<const bf16x8*>((char*)base+(off+m*16+fr)*BK*2+fq*16);};
  f32x4 acc[4][4]={};bf16x8 At[4],Btl[4];
  for(int t=0;t<K/BK;++t){
    stage(t);asm volatile("s_waitcnt vmcnt(0)":::"memory");__syncthreads();
    ld(At,SA,wr*64);ld(Btl,SB,wc*64);
    asm volatile("s_waitcnt lgkmcnt(0)":::"memory");__builtin_amdgcn_sched_barrier(0);
    for(int m=0;m<4;++m)for(int n=0;n<4;++n)
      acc[m][n]=__builtin_amdgcn_mfma_f32_16x16x32_bf16(At[m],Btl[n],acc[m][n],0,0,0);
    __syncthreads();
  }
  for(int m=0;m<4;++m)for(int n=0;n<4;++n)for(int j=0;j<4;++j)
    C[(long)(brow+wr*64+m*16+fq*4+j)*N+(bcol+wc*64+n*16+fr)]=(bf16)(acc[m][n][j]);
}

extern "C" void kernel_launch(void* const* d_in, const int* in_sizes, int n_in,
                              void* d_out, int out_size, void* d_ws, size_t ws_size,
                              hipStream_t stream) {
  const bf16* x=(const bf16*)d_in[0];
  const int* qw=(const int*)d_in[1];
  const unsigned short* lut=(const unsigned short*)d_in[2];
  unsigned short* wt=(unsigned short*)d_ws;
  dequant_kernel<<<dim3(N_/64,K_/64),256,0,stream>>>(qw,lut,wt);
  gemm_128sq<<<(M_/BM)*(N_/BM),256,0,stream>>>(x,(const bf16*)wt,(bf16*)d_out);
}
```

```cpp
#include <hip/hip_runtime.h>
#include <hip/hip_bf16.h>

typedef __attribute__((ext_vector_type(8))) short bf16x8;
typedef __attribute__((ext_vector_type(16))) float f32x16;
typedef __attribute__((ext_vector_type(4))) unsigned u32x4;
typedef __attribute__((ext_vector_type(2))) unsigned u32x2;
typedef __attribute__((ext_vector_type(2))) float f32x2;
typedef __attribute__((ext_vector_type(2))) __bf16 bf2;

constexpr int M_ = 2048, K_ = 1024, N_ = 3584;
constexpr int BM = 128, BN = 224, BK = 64, NSTEP = 16;
constexpr int XSTAGE = BM * BK * 2;
constexpr int WSTAGE = BN * BK * 2;
constexpr int XOFF = 0, WOFF = 4 * XSTAGE;
constexpr int LDS_BYTES = WOFF + 3 * WSTAGE;
constexpr int CSTEP = BN * BK;

__global__ __launch_bounds__(256) void pack_kernel(const int* __restrict__ qw, unsigned char* __restrict__ codes) {
  int u = blockIdx.x * 256 + threadIdx.x;
  int G = u / 896, c4 = u - G * 896;
  const u32x4* p = (const u32x4*)(qw + (long)(G * 8) * N_ + c4 * 4);
  u32x4 r[8];
#pragma unroll
  for (int j = 0; j < 8; ++j) r[j] = __builtin_nontemporal_load(p + j * (N_ / 4));
  unsigned o[8];
#pragma unroll
  for (int e = 0; e < 4; ++e) {
    o[2 * e]     = r[0][e] | (r[1][e] << 8) | (r[2][e] << 16) | (r[3][e] << 24);
    o[2 * e + 1] = r[4][e] | (r[5][e] << 8) | (r[6][e] << 16) | (r[7][e] << 24);
  }
  int n = c4 * 4, nt = n / BN, nn = n - nt * BN, t = G >> 3, g = G & 7;
  int pblk = nn >> 6, cc = nn & 63;
  int off = (nt * NSTEP + t) * CSTEP + pblk * 4096 + (pblk < 3 ? g * 512 : g * 256) + cc * 8;
  u32x4* d = (u32x4*)(codes + off);
  d[0] = u32x4{o[0], o[1], o[2], o[3]};
  d[1] = u32x4{o[4], o[5], o[6], o[7]};
}

__device__ __forceinline__ unsigned pack_bf16(float a, float b) {
  f32x2 v = {a, b};
  bf2 r = __builtin_convertvector(v, bf2);
  return __builtin_bit_cast(unsigned, r);
}

#define LDSP(off) ((__attribute__((address_space(3))) char*)(uintptr_t)(off))
typedef __attribute__((address_space(3))) bf16x8 lds_bf16x8;
typedef __attribute__((address_space(3))) u32x4 lds_u32x4;

template <int C>
__device__ __forceinline__ void consumer(unsigned ldsbase, int r, int lane, unsigned short* __restrict__ out_tile) {
  f32x16 acc[3][2] = {};
  f32x16 accm = {};
  const int row = lane & 31, h = lane >> 5;
  const int f = ((row >> 1) & 7) ^ ((row & 1) << 2);
  const unsigned off0 = row * 128 + ((h ^ f) << 4);
  const unsigned xb = ldsbase + XOFF + r * 8192;
  const unsigned wb = ldsbase + WOFF + C * 12288;
  bf16x8 xa[2][2], wa[2][4];

  auto rd = [&](int set, unsigned xs, unsigned ws, int s) {
    unsigned o = off0 ^ (s << 5);
    unsigned ax = xs + o, aw = ws + o;
#pragma unroll
    for (int j = 0; j < 4; ++j) wa[set][j] = *(const lds_bf16x8*)LDSP(aw + j * 4096);
#pragma unroll
    for (int a = 0; a < 2; ++a) xa[set][a] = *(const lds_bf16x8*)LDSP(ax + a * 4096);
  };
  auto mm = [&](int set) {
#pragma unroll
    for (int j = 0; j < 3; ++j) {
#pragma unroll
      for (int a = 0; a < 2; ++a)
        acc[j][a] = __builtin_amdgcn_mfma_f32_32x32x16_bf16(wa[set][j + C], xa[set][a], acc[j][a], 0, 0, 0);
    }
    accm = __builtin_amdgcn_mfma_f32_32x32x16_bf16(wa[set][C ? 0 : 3], xa[set][C], accm, 0, 0, 0);
  };

  __builtin_amdgcn_s_barrier();
  unsigned xs = xb, ws = wb;
  int wslot = 0;
  rd(0, xs, ws, 0);
#pragma unroll 1
  for (int t = 0; t < NSTEP; ++t) {
    int wn = wslot == 2 ? 0 : wslot + 1;
    unsigned xs2 = xb + (((t + 1) & 3) << 14);
    unsigned ws2 = wb + wn * WSTAGE;
    rd(1, xs, ws, 1);
    mm(0);
    rd(0, xs, ws, 2);
    mm(1);
    rd(1, xs, ws, 3);
    mm(0);
    rd(0, xs2, ws2, 0);
    mm(1);
    __builtin_amdgcn_s_barrier();
    xs = xs2; ws = ws2; wslot = wn;
  }

  auto st = [&](const f32x16& a, int ct, int rt) {
    unsigned short* p = out_tile + (long)(rt * 32 + row) * N_ + ct * 32 + h * 8;
#pragma unroll
    for (int q = 0; q < 4; q += 2) {
      unsigned ax = pack_bf16(a[4 * q], a[4 * q + 1]), ay = pack_bf16(a[4 * q + 2], a[4 * q + 3]);
      unsigned bx = pack_bf16(a[4 * q + 4], a[4 * q + 5]), by = pack_bf16(a[4 * q + 6], a[4 * q + 7]);
      auto r0 = __builtin_amdgcn_permlane32_swap(ax, bx, false, false);
      auto r1 = __builtin_amdgcn_permlane32_swap(ay, by, false, false);
      u32x4 v = {r0[0], r1[0], r0[1], r1[1]};
      *(u32x4*)(p + 8 * q) = v;
    }
  };
#pragma unroll
  for (int j = 0; j < 3; ++j)
#pragma unroll
    for (int a = 0; a < 2; ++a) st(acc[j][a], C * 4 + j, 2 * r + a);
  st(accm, 3, 2 * r + C);
}

struct Lut { unsigned l0, l1, h0, h1; };

__device__ __forceinline__ u32x4 deq8(unsigned s0, unsigned s1, const Lut& L) {
  unsigned lo0 = __builtin_amdgcn_perm(L.l1, L.l0, s0), hi0 = __builtin_amdgcn_perm(L.h1, L.h0, s0);
  unsigned lo1 = __builtin_amdgcn_perm(L.l1, L.l0, s1), hi1 = __builtin_amdgcn_perm(L.h1, L.h0, s1);
  u32x4 o;
  o[0] = __builtin_amdgcn_perm(hi0, lo0, 0x05010400u);
  o[1] = __builtin_amdgcn_perm(hi0, lo0, 0x07030602u);
  o[2] = __builtin_amdgcn_perm(hi1, lo1, 0x05010400u);
  o[3] = __builtin_amdgcn_perm(hi1, lo1, 0x07030602u);
  return o;
}

struct CodeSet { u32x4 a[3]; u32x2 b; };

__device__ __forceinline__ void producer(unsigned ldsbase, int pw, int lane, const unsigned short* __restrict__ xt,
                                         const unsigned char* __restrict__ ct, const unsigned short* __restrict__ lutc) {
  const int pt = pw * 64 + lane, g = pt >> 5, c = pt & 31;
  __amdgpu_buffer_rsrc_t srdX = __builtin_amdgcn_make_buffer_rsrc((void*)xt, (short)0, BM * K_ * 2, 0x00020000);
  __amdgpu_buffer_rsrc_t srdC = __builtin_amdgcn_make_buffer_rsrc((void*)ct, (short)0, NSTEP * CSTEP, 0x00020000);
  const int fE = ((lane >> 4) & 3) ^ (((lane >> 3) & 1) << 2);
  const unsigned xvE = (lane >> 3) * 2048 + (((lane & 7) ^ fE) << 4);
  const unsigned xvO = (lane >> 3) * 2048 + (((lane & 7) ^ fE ^ 4) << 4);
  const unsigned cv0 = pt * 16, cv1 = pt * 16 + 4096, cv2 = pt * 16 + 8192, cv3 = pt * 8 + 12288;

  auto issue_x = [&](int tile) {
    unsigned so = tile < NSTEP ? (unsigned)(pw * 32 * 2048 + tile * 128) : 0x40000000u;
    unsigned m0 = ldsbase + XOFF + ((tile & 3) << 14) + pw * 4096;
#pragma unroll
    for (int j = 0; j < 4; ++j) {
      unsigned mj = m0 + j * 1024, sj = so + j * 16384;
      asm volatile("s_mov_b32 m0, %0\n\ts_nop 0\n\tbuffer_load_dwordx4 %1, %2, %3 offen lds"
                   :: "s"(mj), "v"((j & 1) ? xvO : xvE), "s"(srdX), "s"(sj) : "m0", "memory");
    }
  };
  auto issue_c = [&](CodeSet& S, int tile) {
    unsigned so = (unsigned)tile * CSTEP;
    asm volatile("buffer_load_dwordx4 %0, %1, %2, %3 offen" : "=v"(S.a[0]) : "v"(cv0), "s"(srdC), "s"(so) : "memory");
    asm volatile("buffer_load_dwordx4 %0, %1, %2, %3 offen" : "=v"(S.a[1]) : "v"(cv1), "s"(srdC), "s"(so) : "memory");
    asm volatile("buffer_load_dwordx4 %0, %1, %2, %3 offen" : "=v"(S.a[2]) : "v"(cv2), "s"(srdC), "s"(so) : "memory");
    asm volatile("buffer_load_dwordx2 %0, %1, %2, %3 offen" : "=v"(S.b) : "v"(cv3), "s"(srdC), "s"(so) : "memory");
  };

  CodeSet S0, S1, S2, S3;
  issue_c(S0, 0); issue_x(0); issue_c(S1, 1); issue_x(1);
  issue_c(S2, 2); issue_x(2); issue_c(S3, 3);

  Lut L[7];
  {
    unsigned v[3][8]; unsigned short w[8];
#pragma unroll
    for (int l = 0; l < 8; ++l) {
#pragma unroll
      for (int p = 0; p < 3; ++p) v[p][l] = *(const unsigned*)(lutc + l * N_ + 64 * p + 2 * c);
      w[l] = lutc[l * N_ + 192 + c];
    }
#pragma unroll
    for (int p = 0; p < 3; ++p) {
      unsigned a01 = __builtin_amdgcn_perm(v[p][1], v[p][0], 0x05040100u), b01 = __builtin_amdgcn_perm(v[p][1], v[p][0], 0x07060302u);
      unsigned a23 = __builtin_amdgcn_perm(v[p][3], v[p][2], 0x05040100u), b23 = __builtin_amdgcn_perm(v[p][3], v[p][2], 0x07060302u);
      unsigned a45 = __builtin_amdgcn_perm(v[p][5], v[p][4], 0x05040100u), b45 = __builtin_amdgcn_perm(v[p][5], v[p][4], 0x07060302u);
      unsigned a67 = __builtin_amdgcn_perm(v[p][7], v[p][6], 0x05040100u), b67 = __builtin_amdgcn_perm(v[p][7], v[p][6], 0x07060302u);
      L[2 * p].l0 = __builtin_amdgcn_perm(a23, a01, 0x06040200u); L[2 * p].h0 = __builtin_amdgcn_perm(a23, a01, 0x07050301u);
      L[2 * p].l1 = __builtin_amdgcn_perm(a67, a45, 0x06040200u); L[2 * p].h1 = __builtin_amdgcn_perm(a67, a45, 0x07050301u);
      L[2 * p + 1].l0 = __builtin_amdgcn_perm(b23, b01, 0x06040200u); L[2 * p + 1].h0 = __builtin_amdgcn_perm(b23, b01, 0x07050301u);
      L[2 * p + 1].l1 = __builtin_amdgcn_perm(b67, b45, 0x06040200u); L[2 * p + 1].h1 = __builtin_amdgcn_perm(b67, b45, 0x07050301u);
    }
    unsigned a01 = w[0] | ((unsigned)w[1] << 16), a23 = w[2] | ((unsigned)w[3] << 16);
    unsigned a45 = w[4] | ((unsigned)w[5] << 16), a67 = w[6] | ((unsigned)w[7] << 16);
    L[6].l0 = __builtin_amdgcn_perm(a23, a01, 0x06040200u); L[6].h0 = __builtin_amdgcn_perm(a23, a01, 0x07050301u);
    L[6].l1 = __builtin_amdgcn_perm(a67, a45, 0x06040200u); L[6].h1 = __builtin_amdgcn_perm(a67, a45, 0x07050301u);
  }
  const unsigned wa0 = ldsbase + WOFF + (2 * c) * 128 + ((g ^ (c & 7)) << 4);
  const unsigned wa1 = ldsbase + WOFF + (2 * c + 1) * 128 + ((g ^ (c & 7) ^ 4) << 4);
  const int fl = ((c >> 1) & 7) ^ ((c & 1) << 2);
  const unsigned wa2 = ldsbase + WOFF + (192 + c) * 128 + ((g ^ fl) << 4);

  auto dequant = [&](CodeSet& S, int wslot) {
    unsigned so = wslot * WSTAGE;
    unsigned a0 = wa0 + so, a1 = wa1 + so, a2 = wa2 + so;
#pragma unroll
    for (int p = 0; p < 3; ++p) {
      *(lds_u32x4*)LDSP(a0 + p * 8192) = deq8(S.a[p][0], S.a[p][1], L[2 * p]);
      *(lds_u32x4*)LDSP(a1 + p * 8192) = deq8(S.a[p][2], S.a[p][3], L[2 * p + 1]);
    }
    *(lds_u32x4*)LDSP(a2) = deq8(S.b[0], S.b[1], L[6]);
  };
#define WAITV(n, S) asm volatile("s_waitcnt vmcnt(" #n ")" : "+v"(S.a[0]), "+v"(S.a[1]), "+v"(S.a[2]), "+v"(S.b) :: "memory")

  asm volatile("s_waitcnt vmcnt(0)" ::: "memory");
  WAITV(0, S0); WAITV(0, S1); WAITV(0, S2); WAITV(0, S3);
  dequant(S0, 0);
  dequant(S1, 1);
  asm volatile("s_waitcnt lgkmcnt(0)" ::: "memory");
  __builtin_amdgcn_s_barrier();

  int wslot = 2;
  auto iter = [&](int t, CodeSet& Scur, CodeSet& Snew) {
    issue_x(t + 3);
    issue_c(Snew, t + 4);
    WAITV(16, Scur);
    if (t + 2 < NSTEP) dequant(Scur, wslot);
    wslot = wslot == 2 ? 0 : wslot + 1;
    asm volatile("s_waitcnt vmcnt(12) lgkmcnt(0)" ::: "memory");
    __builtin_amdgcn_s_barrier();
  };
#pragma unroll 1
  for (int t = 0; t < NSTEP; t += 4) {
    iter(t, S2, S0);
    iter(t + 1, S3, S1);
    iter(t + 2, S0, S2);
    iter(t + 3, S1, S3);
  }
  asm volatile("s_waitcnt vmcnt(0)" ::: "memory");
}

__global__ __launch_bounds__(512, 2) void gemm_kernel(const unsigned short* __restrict__ x, const unsigned char* __restrict__ codes,
                                                      const unsigned short* __restrict__ lut, unsigned short* __restrict__ out) {
  __shared__ __attribute__((aligned(16))) char lds[LDS_BYTES];
  unsigned ldsbase = (unsigned)(uintptr_t)(__attribute__((address_space(3))) char*)lds;
  const int tid = threadIdx.x, lane = tid & 63;
  const int wid = __builtin_amdgcn_readfirstlane(tid >> 6);
  const int bid = blockIdx.x, xcd = bid & 7, idx = bid >> 3;
  const int mt = (xcd & 3) * 4 + (idx & 3), nt = (xcd >> 2) * 8 + (idx >> 2);
  if (wid < 4) {
    unsigned short* ot = out + (long)(mt * BM) * N_ + nt * BN;
    if (wid & 1) consumer<1>(ldsbase, wid >> 1, lane, ot);
    else consumer<0>(ldsbase, wid >> 1, lane, ot);
  } else {
    producer(ldsbase, wid - 4, lane, x + (long)(mt * BM) * K_, codes + (long)nt * NSTEP * CSTEP, lut + nt * BN);
  }
}

extern "C" void kernel_launch(void* const* d_in, const int* in_sizes, int n_in, void* d_out, int out_size, void* d_ws,
                              size_t ws_size, hipStream_t stream) {
  const unsigned short* x = (const unsigned short*)d_in[0];
  const int* qw = (const int*)d_in[1];
  const unsigned short* lut = (const unsigned short*)d_in[2];
  unsigned char* codes = (unsigned char*)d_ws;
  pack_kernel<<<448, 256, 0, stream>>>(qw, codes);
  gemm_kernel<<<256, 512, 0, stream>>>(x, codes, lut, (unsigned short*)d_out);
}
```

```cpp
#include <hip/hip_runtime.h>
#include <hip/hip_bf16.h>

typedef __attribute__((ext_vector_type(4))) float f32x4;
typedef __attribute__((ext_vector_type(4))) unsigned u32x4;
typedef __attribute__((ext_vector_type(2))) unsigned u32x2;
typedef __attribute__((address_space(3))) u32x4 lds_u32x4;
typedef __attribute__((address_space(3))) u32x2 lds_u32x2;
typedef __attribute__((address_space(1))) unsigned long long gu64;

constexpr int M_ = 2048, K_ = 1024, N_ = 3584;
constexpr int BM = 128, BN = 224, BK = 64, NSTEP = 16;
constexpr int XSTAGE = BM * BK * 2;
constexpr int CSTAGE = BN * BK;
constexpr int XOFF = 0, COFF = 4 * XSTAGE;
constexpr int SCROFF = COFF + 4 * CSTAGE;
constexpr int TAGOFF = SCROFF + 2 * 3584;
constexpr int LDS_BYTES = TAGOFF + 16;
constexpr int OSTRIDE = 464;

constexpr int WS_E = 0;
constexpr int WS_FLAGS = 4096;
constexpr int FSTRIDE = 8;
constexpr int WS_CODES = 262144;
constexpr unsigned SPIN_CAP = 400000;

#define RLX_AGENT __ATOMIC_RELAXED, __HIP_MEMORY_SCOPE_AGENT
#define LDSP(off) ((__attribute__((address_space(3))) char*)(uintptr_t)(off))
#define M16(acc, w, b) asm volatile("v_mfma_f32_16x16x32_bf16 %0, %1, %2, %0" : "+v"(acc) : "v"(u32x4{w[0], w[1], w[2], w[3]}), "v"(b))
#define DSR(dst, addr, off) asm volatile("ds_read_b128 %0, %1 offset:%2" : "=v"(dst) : "v"(addr), "i"(off))
#define LGKM(n) asm volatile("s_waitcnt lgkmcnt(" #n ")" ::: "memory")
#define VMC(n) asm volatile("s_waitcnt vmcnt(" #n ")" ::: "memory")
#define BAR() do { asm volatile("" ::: "memory"); __builtin_amdgcn_s_barrier(); asm volatile("" ::: "memory"); } while (0)
#define SB() __builtin_amdgcn_sched_barrier(0)
#define PERMV(d, a, b, s) asm volatile("v_perm_b32 %0, %1, %2, %3" : "=v"(d) : "v"(a), "v"(b), "v"(s))
#define PERMS(d, a, b, s) asm volatile("v_perm_b32 %0, %1, %2, %3" : "=v"(d) : "v"(a), "v"(b), "s"(s))
#define DMA(m0v, voff, srd, soff) asm volatile("s_mov_b32 m0, %0\n\ts_nop 0\n\tbuffer_load_dwordx4 %1, %2, %3 offen lds" \
                   :: "s"(m0v), "v"(voff), "s"(srd), "s"(soff) : "memory")
#define DMA4_SC1(m0v, voff, srd, soff) asm volatile("s_mov_b32 m0, %0\n\ts_nop 0\n\tbuffer_load_dwordx4 %1, %2, %3 offen sc1 lds\n\t" \
   "buffer_load_dwordx4 %1, %2, %3 offen offset:1024 sc1 lds\n\tbuffer_load_dwordx4 %1, %2, %3 offen offset:2048 sc1 lds\n\t" \
   "buffer_load_dwordx4 %1, %2, %3 offen offset:3072 sc1 lds" :: "s"(m0v), "v"(voff), "s"(srd), "s"(soff) : "memory")
#define DMA2_SC1(m0v, voff, srd, soff) asm volatile("s_mov_b32 m0, %0\n\ts_nop 0\n\tbuffer_load_dwordx4 %1, %2, %3 offen sc1 lds\n\t" \
   "buffer_load_dwordx4 %1, %2, %3 offen offset:1024 sc1 lds" :: "s"(m0v), "v"(voff), "s"(srd), "s"(soff) : "memory")

struct Lut { unsigned l0, l1, h0, h1; };
struct Unit { u32x4 r[16]; };

__device__ __forceinline__ __amdgpu_buffer_rsrc_t code_rsrc(unsigned char* ws, int nt) {
  return __builtin_amdgcn_make_buffer_rsrc((void*)(ws + WS_CODES + (long)nt * NSTEP * CSTAGE), (short)0, NSTEP * CSTAGE, 0x00020000);
}
__device__ __forceinline__ void unit_load(Unit& U, __amdgpu_buffer_rsrc_t srdQ, unsigned voff, int row0) {
#pragma unroll
  for (int j = 0; j < 16; ++j) {
    unsigned so = (unsigned)(row0 + j) * (N_ * 4);
    asm volatile("buffer_load_dwordx4 %0, %1, %2, %3 offen nt" : "=v"(U.r[j]) : "v"(voff), "s"(srdQ), "s"(so) : "memory");
  }
}
#define UNIT_WAIT(n, U) asm volatile("s_waitcnt vmcnt(" #n ")" \
   : "+v"(U.r[0]), "+v"(U.r[1]), "+v"(U.r[2]), "+v"(U.r[3]), "+v"(U.r[4]), "+v"(U.r[5]), "+v"(U.r[6]), "+v"(U.r[7]), \
     "+v"(U.r[8]), "+v"(U.r[9]), "+v"(U.r[10]), "+v"(U.r[11]), "+v"(U.r[12]), "+v"(U.r[13]), "+v"(U.r[14]), "+v"(U.r[15]) :: "memory")
template <int E0, int E1>
__device__ __forceinline__ void unit_pack(const Unit& U, unsigned dst) {
#pragma unroll
  for (int e = E0; e < E1; ++e) {
    u32x4 o;
#pragma unroll
    for (int dd = 0; dd < 4; ++dd)
      o[dd] = U.r[4 * dd][e] | (U.r[4 * dd + 1][e] << 8) | (U.r[4 * dd + 2][e] << 16) | (U.r[4 * dd + 3][e] << 24);
    *(lds_u32x4*)LDSP(dst + e * 16) = o;
  }
}

__device__ __forceinline__ void consumer(unsigned ldsbase, int pr, int h, int lane, int mt, int nt, __amdgpu_buffer_rsrc_t srdQ,
                                         const unsigned short* __restrict__ lutc, unsigned char* __restrict__ ws,
                                         const unsigned short* __restrict__ xt) {
  const int w = pr * 2 + h;
  const int row = lane & 15, q = lane >> 4;
  u32x2 ev;
  {
    const unsigned char* ep = ws + WS_E;
    asm volatile("global_load_dwordx2 %0, %1, off sc1" : "=v"(ev) : "v"(ep) : "memory");
  }
  const int col = 56 * w + (lane < 56 ? lane : 55);
  unsigned c[16];
  {
    const unsigned voff = col * 4;
#pragma unroll
    for (int j = 0; j < 16; ++j) {
      unsigned so = (unsigned)(mt * 16 + j) * (N_ * 4);
      asm volatile("buffer_load_dword %0, %1, %2, %3 offen nt" : "=v"(c[j]) : "v"(voff), "s"(srdQ), "s"(so) : "memory");
    }
  }
  unsigned wl[4][2];
#pragma unroll
  for (int j = 0; j < 4; ++j) {
    const int tile = pr * 7 + (j == 3 ? 3 : 4 * h + j);
#pragma unroll
    for (int l = 0; l < 2; ++l) {
      const unsigned short* a = lutc + (2 * q + l) * N_ + tile * 16 + row;
      asm volatile("global_load_ushort %0, %1, off" : "=v"(wl[j][l]) : "v"(a) : "memory");
    }
  }
  {
    __amdgpu_buffer_rsrc_t srdX = __builtin_amdgcn_make_buffer_rsrc((void*)xt, (short)0, BM * K_ * 2, 0x00020000);
    const int fE = ((q & 1) << 1) | (q >> 1);
    const unsigned xvE = (lane >> 3) * 2048 + (((lane & 7) ^ fE) << 4);
    const unsigned xvO = (lane >> 3) * 2048 + (((lane & 7) ^ fE ^ 4) << 4);
#pragma unroll
    for (int t = 0; t < 2; ++t)
#pragma unroll
      for (int j = 0; j < 4; ++j)
        DMA(ldsbase + XOFF + t * XSTAGE + w * 4096 + j * 1024, (j & 1) ? xvO : xvE, srdX, (unsigned)((w * 32 + j * 8) * 2048 + t * 128));
  }
  __amdgpu_buffer_rsrc_t srdC = code_rsrc(ws, nt);
  const unsigned coff = ((mt >> 2) * 14 + (col >> 4)) * 1024 + ((mt & 3) * 16 + (col & 15)) * 16;
  f32x4 acc[3][8], accm[4];
#pragma unroll
  for (int a = 0; a < 8; ++a) {
#pragma unroll
    for (int j = 0; j < 3; ++j) { acc[j][a] = f32x4{0.f, 0.f, 0.f, 0.f}; asm volatile("" : "+v"(acc[j][a])); }
    if (a < 4) { accm[a] = f32x4{0.f, 0.f, 0.f, 0.f}; asm volatile("" : "+v"(accm[a])); }
  }
  asm volatile("s_waitcnt vmcnt(32)" : "+v"(ev) :: "memory");
  const unsigned long long tag = ((unsigned long long)(unsigned)__builtin_amdgcn_readfirstlane(ev[1]) << 32 |
                                  (unsigned)__builtin_amdgcn_readfirstlane(ev[0])) + 1ull;
  unsigned dtag[4];
#pragma unroll
  for (int d = 0; d < 4; ++d)
    dtag[d] = (((unsigned)tag & 31u) << 3) | (((unsigned)(tag >> (5 + 10 * d)) & 31u) << 11) |
              (((unsigned)(tag >> (10 + 10 * d)) & 31u) << 19) | ((~(unsigned)tag & 31u) << 27);
  asm volatile("" : "+s"(dtag[0]), "+s"(dtag[1]), "+s"(dtag[2]), "+s"(dtag[3]));
  asm volatile("s_waitcnt vmcnt(16)"
               : "+v"(c[0]), "+v"(c[1]), "+v"(c[2]), "+v"(c[3]), "+v"(c[4]), "+v"(c[5]), "+v"(c[6]), "+v"(c[7]),
                 "+v"(c[8]), "+v"(c[9]), "+v"(c[10]), "+v"(c[11]), "+v"(c[12]), "+v"(c[13]), "+v"(c[14]), "+v"(c[15]) :: "memory");
  {
    u32x4 o;
#pragma unroll
    for (int dd = 0; dd < 4; ++dd)
      o[dd] = (c[4 * dd] | (c[4 * dd + 1] << 8) | (c[4 * dd + 2] << 16) | (c[4 * dd + 3] << 24)) | (mt < 8 ? dtag[dd] : 0u);
    __builtin_amdgcn_raw_buffer_store_b128(o, srdC, coff, 0, 16  );
  }
  if (w == 0) *(lds_u32x2*)LDSP(ldsbase + TAGOFF) = u32x2{(unsigned)tag, (unsigned)(tag >> 32)};
  asm volatile("s_waitcnt vmcnt(9)"
               : "+v"(wl[0][0]), "+v"(wl[0][1]), "+v"(wl[1][0]), "+v"(wl[1][1]), "+v"(wl[2][0]), "+v"(wl[2][1]), "+v"(wl[3][0]), "+v"(wl[3][1]) :: "memory");
  Lut L[4];
#pragma unroll
  for (int j = 0; j < 4; ++j) {
    const int pj = (int)(wl[j][0] | (wl[j][1] << 16));
    unsigned a01 = __builtin_amdgcn_ds_bpermute(row * 4, pj), a23 = __builtin_amdgcn_ds_bpermute(row * 4 + 64, pj);
    unsigned a45 = __builtin_amdgcn_ds_bpermute(row * 4 + 128, pj), a67 = __builtin_amdgcn_ds_bpermute(row * 4 + 192, pj);
    L[j].l0 = __builtin_amdgcn_perm(a23, a01, 0x06040200u); L[j].h0 = __builtin_amdgcn_perm(a23, a01, 0x07050301u);
    L[j].l1 = __builtin_amdgcn_perm(a67, a45, 0x06040200u); L[j].h1 = __builtin_amdgcn_perm(a67, a45, 0x07050301u);
    asm volatile("" : "+v"(L[j].l0), "+v"(L[j].h0), "+v"(L[j].l1), "+v"(L[j].h1));
  }
  const int i8 = (row >> 1) & 7;
  const int f = (i8 & 4) | ((i8 & 1) << 1) | ((i8 >> 1) & 1);
  const unsigned off0 = row * 128 + (((2 * q) ^ f) << 4);
  unsigned axA0 = ldsbase + XOFF + h * 8192 + off0, axA1 = axA0 ^ 16;
  unsigned axB0 = ldsbase + XOFF + (1 - h) * 8192 + off0, axB1 = axB0 ^ 16;
  unsigned acF = ldsbase + COFF + (pr * 7 + 4 * h) * 1024 + lane * 16;
  unsigned acM = ldsbase + COFF + (pr * 7 + 3) * 1024 + lane * 16;
  asm volatile("" : "+v"(axA0), "+v"(axA1), "+v"(axB0), "+v"(axB1), "+v"(acF), "+v"(acM));
  const unsigned selA = 0x05010400u, selB = 0x07030602u;
  u32x4 X[8], Y[4], Cd[4];
  unsigned W0[4][4], W1[4][4], tl0, th0, tl1, th1;

#pragma unroll
  for (int a = 0; a < 4; ++a) {
    X[a] = u32x4{0u, 0u, 0u, 0u}; Y[a] = u32x4{0u, 0u, 0u, 0u};
    asm volatile("" : "+v"(X[a]), "+v"(Y[a]));
#pragma unroll
    for (int e = 0; e < 4; ++e) { W1[a][e] = 0u; asm volatile("" : "+v"(W1[a][e])); }
  }
  {
    const unsigned gF = (pr * 7 + 4 * h) * 1024 + lane * 16, gM = (pr * 7 + 3) * 1024 + lane * 16;
    u32x4 C0[4], C1[4];
    auto fetch = [&]() {
      asm volatile("buffer_load_dwordx4 %0, %1, %2, 0 offen sc1" : "=v"(C0[0]) : "v"(gF), "s"(srdC) : "memory");
      asm volatile("buffer_load_dwordx4 %0, %1, %2, 0 offen offset:1024 sc1" : "=v"(C0[1]) : "v"(gF), "s"(srdC) : "memory");
      asm volatile("buffer_load_dwordx4 %0, %1, %2, 0 offen offset:2048 sc1" : "=v"(C0[2]) : "v"(gF), "s"(srdC) : "memory");
      asm volatile("buffer_load_dwordx4 %0, %1, %2, 0 offen sc1" : "=v"(C0[3]) : "v"(gM), "s"(srdC) : "memory");
      const unsigned so = CSTAGE;
      asm volatile("buffer_load_dwordx4 %0, %1, %2, %3 offen sc1" : "=v"(C1[0]) : "v"(gF), "s"(srdC), "s"(so) : "memory");
      asm volatile("buffer_load_dwordx4 %0, %1, %2, %3 offen offset:1024 sc1" : "=v"(C1[1]) : "v"(gF), "s"(srdC), "s"(so) : "memory");
      asm volatile("buffer_load_dwordx4 %0, %1, %2, %3 offen offset:2048 sc1" : "=v"(C1[2]) : "v"(gF), "s"(srdC), "s"(so) : "memory");
      asm volatile("buffer_load_dwordx4 %0, %1, %2, %3 offen sc1" : "=v"(C1[3]) : "v"(gM), "s"(srdC), "s"(so) : "memory");
    };
    VMC(0);
    fetch();
    LGKM(0);
    BAR();
    asm volatile("s_waitcnt vmcnt(0)" : "+v"(C0[0]), "+v"(C0[1]), "+v"(C0[2]), "+v"(C0[3]), "+v"(C1[0]), "+v"(C1[1]), "+v"(C1[2]), "+v"(C1[3]) :: "memory");
    u32x4 T0[4], T1[4];
#pragma unroll 1
    for (unsigned spins = 0;; ++spins) {
      unsigned bad = 0;
#pragma unroll
      for (int j = 0; j < 4; ++j) {
#pragma unroll
        for (int d = 0; d < 4; ++d) { T0[j][d] = C0[j][d] ^ dtag[d]; T1[j][d] = C1[j][d] ^ dtag[d]; bad |= T0[j][d] | T1[j][d]; }
      }
      if (__all((bad & 0xF8F8F8F8u) == 0u) || spins >= SPIN_CAP) break;
      const unsigned so = CSTAGE;
      asm volatile("buffer_load_dwordx4 %0, %8, %10, 0 offen sc1\n\tbuffer_load_dwordx4 %1, %8, %10, 0 offen offset:1024 sc1\n\t"
                   "buffer_load_dwordx4 %2, %8, %10, 0 offen offset:2048 sc1\n\tbuffer_load_dwordx4 %3, %9, %10, 0 offen sc1\n\t"
                   "buffer_load_dwordx4 %4, %8, %10, %11 offen sc1\n\tbuffer_load_dwordx4 %5, %8, %10, %11 offen offset:1024 sc1\n\t"
                   "buffer_load_dwordx4 %6, %8, %10, %11 offen offset:2048 sc1\n\tbuffer_load_dwordx4 %7, %9, %10, %11 offen sc1\n\t"
                   "s_waitcnt vmcnt(0)"
                   : "=&v"(C0[0]), "=&v"(C0[1]), "=&v"(C0[2]), "=&v"(C0[3]), "=&v"(C1[0]), "=&v"(C1[1]), "=&v"(C1[2]), "=&v"(C1[3])
                   : "v"(gF), "v"(gM), "s"(srdC), "s"(so) : "memory");
    }
    *(lds_u32x4*)LDSP(acF) = T0[0]; *(lds_u32x4*)LDSP(acF + 1024) = T0[1]; *(lds_u32x4*)LDSP(acF + 2048) = T0[2];
    *(lds_u32x4*)LDSP(acM) = T0[3];
    *(lds_u32x4*)LDSP(acF + CSTAGE) = T1[0]; *(lds_u32x4*)LDSP(acF + CSTAGE + 1024) = T1[1]; *(lds_u32x4*)LDSP(acF + CSTAGE + 2048) = T1[2];
    *(lds_u32x4*)LDSP(acM + CSTAGE) = T1[3];
  }
#pragma unroll 1
  for (int t = 0; t < NSTEP; t += 4) {
    LGKM(0); BAR();
    M16(acc[0][0], W1[0], X[0]);
    DSR(Cd[0], acF, 0);
    M16(acc[1][0], W1[1], X[0]);
    DSR(Cd[1], acF, 1024);
    M16(acc[2][0], W1[2], X[0]);
    DSR(Cd[2], acF, 2048);
    M16(accm[0], W1[3], X[0]);
    DSR(Cd[3], acM, 0);
    M16(acc[0][1], W1[0], X[1]);
    DSR(X[4], axB0, 0);
    M16(acc[1][1], W1[1], X[1]);
    DSR(X[5], axB0, 2048);
    M16(acc[2][1], W1[2], X[1]);
    DSR(X[6], axB0, 4096);
    LGKM(5);
    PERMV(tl0, L[0].l1, L[0].l0, Cd[0][0]);
    M16(accm[1], W1[3], X[1]);
    DSR(X[1], axA0, 2048);
    PERMV(th0, L[0].h1, L[0].h0, Cd[0][0]);
    M16(acc[0][2], W1[0], X[2]);
    DSR(X[7], axB0, 6144);
    PERMV(tl1, L[0].l1, L[0].l0, Cd[0][1]);
    M16(acc[1][2], W1[1], X[2]);
    DSR(X[0], axA0, 0);
    PERMV(th1, L[0].h1, L[0].h0, Cd[0][1]);
    M16(acc[2][2], W1[2], X[2]);
    PERMS(W0[0][0], th0, tl0, selA);
    PERMS(W0[0][1], th0, tl0, selB);
    M16(accm[2], W1[3], X[2]);
    DSR(X[2], axA0, 4096);
    PERMS(W0[0][2], th1, tl1, selA);
    M16(acc[0][3], W1[0], X[3]);
    PERMS(W0[0][3], th1, tl1, selB);
    PERMV(tl0, L[1].l1, L[1].l0, Cd[1][0]);
    M16(acc[1][3], W1[1], X[3]);
    PERMV(th0, L[1].h1, L[1].h0, Cd[1][0]);
    PERMV(tl1, L[1].l1, L[1].l0, Cd[1][1]);
    M16(acc[2][3], W1[2], X[3]);
    PERMV(th1, L[1].h1, L[1].h0, Cd[1][1]);
    PERMS(W0[1][0], th0, tl0, selA);
    M16(accm[3], W1[3], X[3]);
    DSR(X[3], axA0, 6144);
    PERMS(W0[1][1], th0, tl0, selB);
    M16(acc[0][4], W1[0], Y[0]);
    PERMS(W0[1][2], th1, tl1, selA);
    PERMS(W0[1][3], th1, tl1, selB);
    M16(acc[1][4], W1[1], Y[0]);
    LGKM(8);
    PERMV(tl0, L[2].l1, L[2].l0, Cd[2][0]);
    PERMV(th0, L[2].h1, L[2].h0, Cd[2][0]);
    M16(acc[2][4], W1[2], Y[0]);
    PERMV(tl1, L[2].l1, L[2].l0, Cd[2][1]);
    PERMV(th1, L[2].h1, L[2].h0, Cd[2][1]);
    M16(acc[0][5], W1[0], Y[1]);
    PERMS(W0[2][0], th0, tl0, selA);
    PERMS(W0[2][1], th0, tl0, selB);
    M16(acc[1][5], W1[1], Y[1]);
    PERMS(W0[2][2], th1, tl1, selA);
    PERMS(W0[2][3], th1, tl1, selB);
    M16(acc[2][5], W1[2], Y[1]);
    PERMV(tl0, L[3].l1, L[3].l0, Cd[3][0]);
    PERMV(th0, L[3].h1, L[3].h0, Cd[3][0]);
    M16(acc[0][6], W1[0], Y[2]);
    PERMV(tl1, L[3].l1, L[3].l0, Cd[3][1]);
    PERMV(th1, L[3].h1, L[3].h0, Cd[3][1]);
    M16(acc[1][6], W1[1], Y[2]);
    PERMS(W0[3][0], th0, tl0, selA);
    M16(acc[2][6], W1[2], Y[2]);
    PERMS(W0[3][1], th0, tl0, selB);
    M16(acc[0][7], W1[0], Y[3]);
    PERMS(W0[3][2], th1, tl1, selA);
    M16(acc[1][7], W1[1], Y[3]);
    PERMS(W0[3][3], th1, tl1, selB);
    M16(acc[2][7], W1[2], Y[3]);
    LGKM(0);
    M16(acc[0][0], W0[0], X[0]);
    DSR(Y[0], axB1, 0);
    PERMV(tl0, L[0].l1, L[0].l0, Cd[0][2]);
    M16(acc[1][0], W0[1], X[0]);
    DSR(Y[1], axB1, 2048);
    PERMV(th0, L[0].h1, L[0].h0, Cd[0][2]);
    M16(acc[2][0], W0[2], X[0]);
    DSR(Y[2], axB1, 4096);
    PERMV(tl1, L[0].l1, L[0].l0, Cd[0][3]);
    M16(accm[0], W0[3], X[0]);
    DSR(X[0], axA1, 0);
    PERMV(th1, L[0].h1, L[0].h0, Cd[0][3]);
    M16(acc[0][1], W0[0], X[1]);
    DSR(Y[3], axB1, 6144);
    PERMS(W1[0][0], th0, tl0, selA);
    M16(acc[1][1], W0[1], X[1]);
    PERMS(W1[0][1], th0, tl0, selB);
    PERMS(W1[0][2], th1, tl1, selA);
    M16(acc[2][1], W0[2], X[1]);
    PERMS(W1[0][3], th1, tl1, selB);
    PERMV(tl0, L[1].l1, L[1].l0, Cd[1][2]);
    M16(accm[1], W0[3], X[1]);
    DSR(X[1], axA1, 2048);
    PERMV(th0, L[1].h1, L[1].h0, Cd[1][2]);
    M16(acc[0][2], W0[0], X[2]);
    PERMV(tl1, L[1].l1, L[1].l0, Cd[1][3]);
    PERMV(th1, L[1].h1, L[1].h0, Cd[1][3]);
    M16(acc[1][2], W0[1], X[2]);
    PERMS(W1[1][0], th0, tl0, selA);
    PERMS(W1[1][1], th0, tl0, selB);
    M16(acc[2][2], W0[2], X[2]);
    PERMS(W1[1][2], th1, tl1, selA);
    PERMS(W1[1][3], th1, tl1, selB);
    M16(accm[2], W0[3], X[2]);
    DSR(X[2], axA1, 4096);
    PERMV(tl0, L[2].l1, L[2].l0, Cd[2][2]);
    M16(acc[0][3], W0[0], X[3]);
    PERMV(th0, L[2].h1, L[2].h0, Cd[2][2]);
    M16(acc[1][3], W0[1], X[3]);
    PERMV(tl1, L[2].l1, L[2].l0, Cd[2][3]);
    M16(acc[2][3], W0[2], X[3]);
    PERMV(th1, L[2].h1, L[2].h0, Cd[2][3]);
    M16(accm[3], W0[3], X[3]);
    DSR(X[3], axA1, 6144);
    PERMS(W1[2][0], th0, tl0, selA);
    LGKM(11);
    M16(acc[0][4], W0[0], X[4]);
    PERMS(W1[2][1], th0, tl0, selB);
    M16(acc[1][4], W0[1], X[4]);
    PERMS(W1[2][2], th1, tl1, selA);
    M16(acc[2][4], W0[2], X[4]);
    PERMS(W1[2][3], th1, tl1, selB);
    M16(acc[0][5], W0[0], X[5]);
    PERMV(tl0, L[3].l1, L[3].l0, Cd[3][2]);
    M16(acc[1][5], W0[1], X[5]);
    PERMV(th0, L[3].h1, L[3].h0, Cd[3][2]);
    M16(acc[2][5], W0[2], X[5]);
    PERMV(tl1, L[3].l1, L[3].l0, Cd[3][3]);
    M16(acc[0][6], W0[0], X[6]);
    PERMV(th1, L[3].h1, L[3].h0, Cd[3][3]);
    M16(acc[1][6], W0[1], X[6]);
    PERMS(W1[3][0], th0, tl0, selA);
    M16(acc[2][6], W0[2], X[6]);
    PERMS(W1[3][1], th0, tl0, selB);
    M16(acc[0][7], W0[0], X[7]);
    PERMS(W1[3][2], th1, tl1, selA);
    M16(acc[1][7], W0[1], X[7]);
    PERMS(W1[3][3], th1, tl1, selB);
    M16(acc[2][7], W0[2], X[7]);
    LGKM(0); BAR();
    M16(acc[0][0], W1[0], X[0]);
    DSR(Cd[0], acF, 14336);
    M16(acc[1][0], W1[1], X[0]);
    DSR(Cd[1], acF, 15360);
    M16(acc[2][0], W1[2], X[0]);
    DSR(Cd[2], acF, 16384);
    M16(accm[0], W1[3], X[0]);
    DSR(Cd[3], acM, 14336);
    M16(acc[0][1], W1[0], X[1]);
    DSR(X[4], axB0, 16384);
    M16(acc[1][1], W1[1], X[1]);
    DSR(X[5], axB0, 18432);
    M16(acc[2][1], W1[2], X[1]);
    DSR(X[6], axB0, 20480);
    LGKM(5);
    PERMV(tl0, L[0].l1, L[0].l0, Cd[0][0]);
    M16(accm[1], W1[3], X[1]);
    DSR(X[1], axA0, 18432);
    PERMV(th0, L[0].h1, L[0].h0, Cd[0][0]);
    M16(acc[0][2], W1[0], X[2]);
    DSR(X[7], axB0, 22528);
    PERMV(tl1, L[0].l1, L[0].l0, Cd[0][1]);
    M16(acc[1][2], W1[1], X[2]);
    DSR(X[0], axA0, 16384);
    PERMV(th1, L[0].h1, L[0].h0, Cd[0][1]);
    M16(acc[2][2], W1[2], X[2]);
    PERMS(W0[0][0], th0, tl0, selA);
    PERMS(W0[0][1], th0, tl0, selB);
    M16(accm[2], W1[3], X[2]);
    DSR(X[2], axA0, 20480);
    PERMS(W0[0][2], th1, tl1, selA);
    M16(acc[0][3], W1[0], X[3]);
    PERMS(W0[0][3], th1, tl1, selB);
    PERMV(tl0, L[1].l1, L[1].l0, Cd[1][0]);
    M16(acc[1][3], W1[1], X[3]);
    PERMV(th0, L[1].h1, L[1].h0, Cd[1][0]);
    PERMV(tl1, L[1].l1, L[1].l0, Cd[1][1]);
    M16(acc[2][3], W1[2], X[3]);
    PERMV(th1, L[1].h1, L[1].h0, Cd[1][1]);
    PERMS(W0[1][0], th0, tl0, selA);
    M16(accm[3], W1[3], X[3]);
    DSR(X[3], axA0, 22528);
    PERMS(W0[1][1], th0, tl0, selB);
    M16(acc[0][4], W1[0], Y[0]);
    PERMS(W0[1][2], th1, tl1, selA);
    PERMS(W0[1][3], th1, tl1, selB);
    M16(acc[1][4], W1[1], Y[0]);
    LGKM(8);
    PERMV(tl0, L[2].l1, L[2].l0, Cd[2][0]);
    PERMV(th0, L[2].h1, L[2].h0, Cd[2][0]);
    M16(acc[2][4], W1[2], Y[0]);
    PERMV(tl1, L[2].l1, L[2].l0, Cd[2][1]);
    PERMV(th1, L[2].h1, L[2].h0, Cd[2][1]);
    M16(acc[0][5], W1[0], Y[1]);
    PERMS(W0[2][0], th0, tl0, selA);
    PERMS(W0[2][1], th0, tl0, selB);
    M16(acc[1][5], W1[1], Y[1]);
    PERMS(W0[2][2], th1, tl1, selA);
    PERMS(W0[2][3], th1, tl1, selB);
    M16(acc[2][5], W1[2], Y[1]);
    PERMV(tl0, L[3].l1, L[3].l0, Cd[3][0]);
    PERMV(th0, L[3].h1, L[3].h0, Cd[3][0]);
    M16(acc[0][6], W1[0], Y[2]);
    PERMV(tl1, L[3].l1, L[3].l0, Cd[3][1]);
    PERMV(th1, L[3].h1, L[3].h0, Cd[3][1]);
    M16(acc[1][6], W1[1], Y[2]);
    PERMS(W0[3][0], th0, tl0, selA);
    M16(acc[2][6], W1[2], Y[2]);
    PERMS(W0[3][1], th0, tl0, selB);
    M16(acc[0][7], W1[0], Y[3]);
    PERMS(W0[3][2], th1, tl1, selA);
    M16(acc[1][7], W1[1], Y[3]);
    PERMS(W0[3][3], th1, tl1, selB);
    M16(acc[2][7], W1[2], Y[3]);
    LGKM(0);
    M16(acc[0][0], W0[0], X[0]);
    DSR(Y[0], axB1, 16384);
    PERMV(tl0, L[0].l1, L[0].l0, Cd[0][2]);
    M16(acc[1][0], W0[1], X[0]);
    DSR(Y[1], axB1, 18432);
    PERMV(th0, L[0].h1, L[0].h0, Cd[0][2]);
    M16(acc[2][0], W0[2], X[0]);
    DSR(Y[2], axB1, 20480);
    PERMV(tl1, L[0].l1, L[0].l0, Cd[0][3]);
    M16(accm[0], W0[3], X[0]);
    DSR(X[0], axA1, 16384);
    PERMV(th1, L[0].h1, L[0].h0, Cd[0][3]);
    M16(acc[0][1], W0[0], X[1]);
    DSR(Y[3], axB1, 22528);
    PERMS(W1[0][0], th0, tl0, selA);
    M16(acc[1][1], W0[1], X[1]);
    PERMS(W1[0][1], th0, tl0, selB);
    PERMS(W1[0][2], th1, tl1, selA);
    M16(acc[2][1], W0[2], X[1]);
    PERMS(W1[0][3], th1, tl1, selB);
    PERMV(tl0, L[1].l1, L[1].l0, Cd[1][2]);
    M16(accm[1], W0[3], X[1]);
    DSR(X[1], axA1, 18432);
    PERMV(th0, L[1].h1, L[1].h0, Cd[1][2]);
    M16(acc[0][2], W0[0], X[2]);
    PERMV(tl1, L[1].l1, L[1].l0, Cd[1][3]);
    PERMV(th1, L[1].h1, L[1].h0, Cd[1][3]);
    M16(acc[1][2], W0[1], X[2]);
    PERMS(W1[1][0], th0, tl0, selA);
    PERMS(W1[1][1], th0, tl0, selB);
    M16(acc[2][2], W0[2], X[2]);
    PERMS(W1[1][2], th1, tl1, selA);
    PERMS(W1[1][3], th1, tl1, selB);
    M16(accm[2], W0[3], X[2]);
    DSR(X[2], axA1, 20480);
    PERMV(tl0, L[2].l1, L[2].l0, Cd[2][2]);
    M16(acc[0][3], W0[0], X[3]);
    PERMV(th0, L[2].h1, L[2].h0, Cd[2][2]);
    M16(acc[1][3], W0[1], X[3]);
    PERMV(tl1, L[2].l1, L[2].l0, Cd[2][3]);
    M16(acc[2][3], W0[2], X[3]);
    PERMV(th1, L[2].h1, L[2].h0, Cd[2][3]);
    M16(accm[3], W0[3], X[3]);
    DSR(X[3], axA1, 22528);
    PERMS(W1[2][0], th0, tl0, selA);
    LGKM(11);
    M16(acc[0][4], W0[0], X[4]);
    PERMS(W1[2][1], th0, tl0, selB);
    M16(acc[1][4], W0[1], X[4]);
    PERMS(W1[2][2], th1, tl1, selA);
    M16(acc[2][4], W0[2], X[4]);
    PERMS(W1[2][3], th1, tl1, selB);
    M16(acc[0][5], W0[0], X[5]);
    PERMV(tl0, L[3].l1, L[3].l0, Cd[3][2]);
    M16(acc[1][5], W0[1], X[5]);
    PERMV(th0, L[3].h1, L[3].h0, Cd[3][2]);
    M16(acc[2][5], W0[2], X[5]);
    PERMV(tl1, L[3].l1, L[3].l0, Cd[3][3]);
    M16(acc[0][6], W0[0], X[6]);
    PERMV(th1, L[3].h1, L[3].h0, Cd[3][3]);
    M16(acc[1][6], W0[1], X[6]);
    PERMS(W1[3][0], th0, tl0, selA);
    M16(acc[2][6], W0[2], X[6]);
    PERMS(W1[3][1], th0, tl0, selB);
    M16(acc[0][7], W0[0], X[7]);
    PERMS(W1[3][2], th1, tl1, selA);
    M16(acc[1][7], W0[1], X[7]);
    PERMS(W1[3][3], th1, tl1, selB);
    M16(acc[2][7], W0[2], X[7]);
    LGKM(0); BAR();
    M16(acc[0][0], W1[0], X[0]);
    DSR(Cd[0], acF, 28672);
    M16(acc[1][0], W1[1], X[0]);
    DSR(Cd[1], acF, 29696);
    M16(acc[2][0], W1[2], X[0]);
    DSR(Cd[2], acF, 30720);
    M16(accm[0], W1[3], X[0]);
    DSR(Cd[3], acM, 28672);
    M16(acc[0][1], W1[0], X[1]);
    DSR(X[4], axB0, 32768);
    M16(acc[1][1], W1[1], X[1]);
    DSR(X[5], axB0, 34816);
    M16(acc[2][1], W1[2], X[1]);
    DSR(X[6], axB0, 36864);
    LGKM(5);
    PERMV(tl0, L[0].l1, L[0].l0, Cd[0][0]);
    M16(accm[1], W1[3], X[1]);
    DSR(X[1], axA0, 34816);
    PERMV(th0, L[0].h1, L[0].h0, Cd[0][0]);
    M16(acc[0][2], W1[0], X[2]);
    DSR(X[7], axB0, 38912);
    PERMV(tl1, L[0].l1, L[0].l0, Cd[0][1]);
    M16(acc[1][2], W1[1], X[2]);
    DSR(X[0], axA0, 32768);
    PERMV(th1, L[0].h1, L[0].h0, Cd[0][1]);
    M16(acc[2][2], W1[2], X[2]);
    PERMS(W0[0][0], th0, tl0, selA);
    PERMS(W0[0][1], th0, tl0, selB);
    M16(accm[2], W1[3], X[2]);
    DSR(X[2], axA0, 36864);
    PERMS(W0[0][2], th1, tl1, selA);
    M16(acc[0][3], W1[0], X[3]);
    PERMS(W0[0][3], th1, tl1, selB);
    PERMV(tl0, L[1].l1, L[1].l0, Cd[1][0]);
    M16(acc[1][3], W1[1], X[3]);
    PERMV(th0, L[1].h1, L[1].h0, Cd[1][0]);
    PERMV(tl1, L[1].l1, L[1].l0, Cd[1][1]);
    M16(acc[2][3], W1[2], X[3]);
    PERMV(th1, L[1].h1, L[1].h0, Cd[1][1]);
    PERMS(W0[1][0], th0, tl0, selA);
    M16(accm[3], W1[3], X[3]);
    DSR(X[3], axA0, 38912);
    PERMS(W0[1][1], th0, tl0, selB);
    M16(acc[0][4], W1[0], Y[0]);
    PERMS(W0[1][2], th1, tl1, selA);
    PERMS(W0[1][3], th1, tl1, selB);
    M16(acc[1][4], W1[1], Y[0]);
    LGKM(8);
    PERMV(tl0, L[2].l1, L[2].l0, Cd[2][0]);
    PERMV(th0, L[2].h1, L[2].h0, Cd[2][0]);
    M16(acc[2][4], W1[2], Y[0]);
    PERMV(tl1, L[2].l1, L[2].l0, Cd[2][1]);
    PERMV(th1, L[2].h1, L[2].h0, Cd[2][1]);
    M16(acc[0][5], W1[0], Y[1]);
    PERMS(W0[2][0], th0, tl0, selA);
    PERMS(W0[2][1], th0, tl0, selB);
    M16(acc[1][5], W1[1], Y[1]);
    PERMS(W0[2][2], th1, tl1, selA);
    PERMS(W0[2][3], th1, tl1, selB);
    M16(acc[2][5], W1[2], Y[1]);
    PERMV(tl0, L[3].l1, L[3].l0, Cd[3][0]);
    PERMV(th0, L[3].h1, L[3].h0, Cd[3][0]);
    M16(acc[0][6], W1[0], Y[2]);
    PERMV(tl1, L[3].l1, L[3].l0, Cd[3][1]);
    PERMV(th1, L[3].h1, L[3].h0, Cd[3][1]);
    M16(acc[1][6], W1[1], Y[2]);
    PERMS(W0[3][0], th0, tl0, selA);
    M16(acc[2][6], W1[2], Y[2]);
    PERMS(W0[3][1], th0, tl0, selB);
    M16(acc[0][7], W1[0], Y[3]);
    PERMS(W0[3][2], th1, tl1, selA);
    M16(acc[1][7], W1[1], Y[3]);
    PERMS(W0[3][3], th1, tl1, selB);
    M16(acc[2][7], W1[2], Y[3]);
    LGKM(0);
    M16(acc[0][0], W0[0], X[0]);
    DSR(Y[0], axB1, 32768);
    PERMV(tl0, L[0].l1, L[0].l0, Cd[0][2]);
    M16(acc[1][0], W0[1], X[0]);
    DSR(Y[1], axB1, 34816);
    PERMV(th0, L[0].h1, L[0].h0, Cd[0][2]);
    M16(acc[2][0], W0[2], X[0]);
    DSR(Y[2], axB1, 36864);
    PERMV(tl1, L[0].l1, L[0].l0, Cd[0][3]);
    M16(accm[0], W0[3], X[0]);
    DSR(X[0], axA1, 32768);
    PERMV(th1, L[0].h1, L[0].h0, Cd[0][3]);
    M16(acc[0][1], W0[0], X[1]);
    DSR(Y[3], axB1, 38912);
    PERMS(W1[0][0], th0, tl0, selA);
    M16(acc[1][1], W0[1], X[1]);
    PERMS(W1[0][1], th0, tl0, selB);
    PERMS(W1[0][2], th1, tl1, selA);
    M16(acc[2][1], W0[2], X[1]);
    PERMS(W1[0][3], th1, tl1, selB);
    PERMV(tl0, L[1].l1, L[1].l0, Cd[1][2]);
    M16(accm[1], W0[3], X[1]);
    DSR(X[1], axA1, 34816);
    PERMV(th0, L[1].h1, L[1].h0, Cd[1][2]);
    M16(acc[0][2], W0[0], X[2]);
    PERMV(tl1, L[1].l1, L[1].l0, Cd[1][3]);
    PERMV(th1, L[1].h1, L[1].h0, Cd[1][3]);
    M16(acc[1][2], W0[1], X[2]);
    PERMS(W1[1][0], th0, tl0, selA);
    PERMS(W1[1][1], th0, tl0, selB);
    M16(acc[2][2], W0[2], X[2]);
    PERMS(W1[1][2], th1, tl1, selA);
    PERMS(W1[1][3], th1, tl1, selB);
    M16(accm[2], W0[3], X[2]);
    DSR(X[2], axA1, 36864);
    PERMV(tl0, L[2].l1, L[2].l0, Cd[2][2]);
    M16(acc[0][3], W0[0], X[3]);
    PERMV(th0, L[2].h1, L[2].h0, Cd[2][2]);
    M16(acc[1][3], W0[1], X[3]);
    PERMV(tl1, L[2].l1, L[2].l0, Cd[2][3]);
    M16(acc[2][3], W0[2], X[3]);
    PERMV(th1, L[2].h1, L[2].h0, Cd[2][3]);
    M16(accm[3], W0[3], X[3]);
    DSR(X[3], axA1, 38912);
    PERMS(W1[2][0], th0, tl0, selA);
    LGKM(11);
    M16(acc[0][4], W0[0], X[4]);
    PERMS(W1[2][1], th0, tl0, selB);
    M16(acc[1][4], W0[1], X[4]);
    PERMS(W1[2][2], th1, tl1, selA);
    M16(acc[2][4], W0[2], X[4]);
    PERMS(W1[2][3], th1, tl1, selB);
    M16(acc[0][5], W0[0], X[5]);
    PERMV(tl0, L[3].l1, L[3].l0, Cd[3][2]);
    M16(acc[1][5], W0[1], X[5]);
    PERMV(th0, L[3].h1, L[3].h0, Cd[3][2]);
    M16(acc[2][5], W0[2], X[5]);
    PERMV(tl1, L[3].l1, L[3].l0, Cd[3][3]);
    M16(acc[0][6], W0[0], X[6]);
    PERMV(th1, L[3].h1, L[3].h0, Cd[3][3]);
    M16(acc[1][6], W0[1], X[6]);
    PERMS(W1[3][0], th0, tl0, selA);
    M16(acc[2][6], W0[2], X[6]);
    PERMS(W1[3][1], th0, tl0, selB);
    M16(acc[0][7], W0[0], X[7]);
    PERMS(W1[3][2], th1, tl1, selA);
    M16(acc[1][7], W0[1], X[7]);
    PERMS(W1[3][3], th1, tl1, selB);
    M16(acc[2][7], W0[2], X[7]);
    LGKM(0); BAR();
    M16(acc[0][0], W1[0], X[0]);
    DSR(Cd[0], acF, 43008);
    M16(acc[1][0], W1[1], X[0]);
    DSR(Cd[1], acF, 44032);
    M16(acc[2][0], W1[2], X[0]);
    DSR(Cd[2], acF, 45056);
    M16(accm[0], W1[3], X[0]);
    DSR(Cd[3], acM, 43008);
    M16(acc[0][1], W1[0], X[1]);
    DSR(X[4], axB0, 49152);
    M16(acc[1][1], W1[1], X[1]);
    DSR(X[5], axB0, 51200);
    M16(acc[2][1], W1[2], X[1]);
    DSR(X[6], axB0, 53248);
    LGKM(5);
    PERMV(tl0, L[0].l1, L[0].l0, Cd[0][0]);
    M16(accm[1], W1[3], X[1]);
    DSR(X[1], axA0, 51200);
    PERMV(th0, L[0].h1, L[0].h0, Cd[0][0]);
    M16(acc[0][2], W1[0], X[2]);
    DSR(X[7], axB0, 55296);
    PERMV(tl1, L[0].l1, L[0].l0, Cd[0][1]);
    M16(acc[1][2], W1[1], X[2]);
    DSR(X[0], axA0, 49152);
    PERMV(th1, L[0].h1, L[0].h0, Cd[0][1]);
    M16(acc[2][2], W1[2], X[2]);
    PERMS(W0[0][0], th0, tl0, selA);
    PERMS(W0[0][1], th0, tl0, selB);
    M16(accm[2], W1[3], X[2]);
    DSR(X[2], axA0, 53248);
    PERMS(W0[0][2], th1, tl1, selA);
    M16(acc[0][3], W1[0], X[3]);
    PERMS(W0[0][3], th1, tl1, selB);
    PERMV(tl0, L[1].l1, L[1].l0, Cd[1][0]);
    M16(acc[1][3], W1[1], X[3]);
    PERMV(th0, L[1].h1, L[1].h0, Cd[1][0]);
    PERMV(tl1, L[1].l1, L[1].l0, Cd[1][1]);
    M16(acc[2][3], W1[2], X[3]);
    PERMV(th1, L[1].h1, L[1].h0, Cd[1][1]);
    PERMS(W0[1][0], th0, tl0, selA);
    M16(accm[3], W1[3], X[3]);
    DSR(X[3], axA0, 55296);
    PERMS(W0[1][1], th0, tl0, selB);
    M16(acc[0][4], W1[0], Y[0]);
    PERMS(W0[1][2], th1, tl1, selA);
    PERMS(W0[1][3], th1, tl1, selB);
    M16(acc[1][4], W1[1], Y[0]);
    LGKM(8);
    PERMV(tl0, L[2].l1, L[2].l0, Cd[2][0]);
    PERMV(th0, L[2].h1, L[2].h0, Cd[2][0]);
    M16(acc[2][4], W1[2], Y[0]);
    PERMV(tl1, L[2].l1, L[2].l0, Cd[2][1]);
    PERMV(th1, L[2].h1, L[2].h0, Cd[2][1]);
    M16(acc[0][5], W1[0], Y[1]);
    PERMS(W0[2][0], th0, tl0, selA);
    PERMS(W0[2][1], th0, tl0, selB);
    M16(acc[1][5], W1[1], Y[1]);
    PERMS(W0[2][2], th1, tl1, selA);
    PERMS(W0[2][3], th1, tl1, selB);
    M16(acc[2][5], W1[2], Y[1]);
    PERMV(tl0, L[3].l1, L[3].l0, Cd[3][0]);
    PERMV(th0, L[3].h1, L[3].h0, Cd[3][0]);
    M16(acc[0][6], W1[0], Y[2]);
    PERMV(tl1, L[3].l1, L[3].l0, Cd[3][1]);
    PERMV(th1, L[3].h1, L[3].h0, Cd[3][1]);
    M16(acc[1][6], W1[1], Y[2]);
    PERMS(W0[3][0], th0, tl0, selA);
    M16(acc[2][6], W1[2], Y[2]);
    PERMS(W0[3][1], th0, tl0, selB);
    M16(acc[0][7], W1[0], Y[3]);
    PERMS(W0[3][2], th1, tl1, selA);
    M16(acc[1][7], W1[1], Y[3]);
    PERMS(W0[3][3], th1, tl1, selB);
    M16(acc[2][7], W1[2], Y[3]);
    LGKM(0);
    M16(acc[0][0], W0[0], X[0]);
    DSR(Y[0], axB1, 49152);
    PERMV(tl0, L[0].l1, L[0].l0, Cd[0][2]);
    M16(acc[1][0], W0[1], X[0]);
    DSR(Y[1], axB1, 51200);
    PERMV(th0, L[0].h1, L[0].h0, Cd[0][2]);
    M16(acc[2][0], W0[2], X[0]);
    DSR(Y[2], axB1, 53248);
    PERMV(tl1, L[0].l1, L[0].l0, Cd[0][3]);
    M16(accm[0], W0[3], X[0]);
    DSR(X[0], axA1, 49152);
    PERMV(th1, L[0].h1, L[0].h0, Cd[0][3]);
    M16(acc[0][1], W0[0], X[1]);
    DSR(Y[3], axB1, 55296);
    PERMS(W1[0][0], th0, tl0, selA);
    M16(acc[1][1], W0[1], X[1]);
    PERMS(W1[0][1], th0, tl0, selB);
    PERMS(W1[0][2], th1, tl1, selA);
    M16(acc[2][1], W0[2], X[1]);
    PERMS(W1[0][3], th1, tl1, selB);
    PERMV(tl0, L[1].l1, L[1].l0, Cd[1][2]);
    M16(accm[1], W0[3], X[1]);
    DSR(X[1], axA1, 51200);
    PERMV(th0, L[1].h1, L[1].h0, Cd[1][2]);
    M16(acc[0][2], W0[0], X[2]);
    PERMV(tl1, L[1].l1, L[1].l0, Cd[1][3]);
    PERMV(th1, L[1].h1, L[1].h0, Cd[1][3]);
    M16(acc[1][2], W0[1], X[2]);
    PERMS(W1[1][0], th0, tl0, selA);
    PERMS(W1[1][1], th0, tl0, selB);
    M16(acc[2][2], W0[2], X[2]);
    PERMS(W1[1][2], th1, tl1, selA);
    PERMS(W1[1][3], th1, tl1, selB);
    M16(accm[2], W0[3], X[2]);
    DSR(X[2], axA1, 53248);
    PERMV(tl0, L[2].l1, L[2].l0, Cd[2][2]);
    M16(acc[0][3], W0[0], X[3]);
    PERMV(th0, L[2].h1, L[2].h0, Cd[2][2]);
    M16(acc[1][3], W0[1], X[3]);
    PERMV(tl1, L[2].l1, L[2].l0, Cd[2][3]);
    M16(acc[2][3], W0[2], X[3]);
    PERMV(th1, L[2].h1, L[2].h0, Cd[2][3]);
    M16(accm[3], W0[3], X[3]);
    DSR(X[3], axA1, 55296);
    PERMS(W1[2][0], th0, tl0, selA);
    LGKM(11);
    M16(acc[0][4], W0[0], X[4]);
    PERMS(W1[2][1], th0, tl0, selB);
    M16(acc[1][4], W0[1], X[4]);
    PERMS(W1[2][2], th1, tl1, selA);
    M16(acc[2][4], W0[2], X[4]);
    PERMS(W1[2][3], th1, tl1, selB);
    M16(acc[0][5], W0[0], X[5]);
    PERMV(tl0, L[3].l1, L[3].l0, Cd[3][2]);
    M16(acc[1][5], W0[1], X[5]);
    PERMV(th0, L[3].h1, L[3].h0, Cd[3][2]);
    M16(acc[2][5], W0[2], X[5]);
    PERMV(tl1, L[3].l1, L[3].l0, Cd[3][3]);
    M16(acc[0][6], W0[0], X[6]);
    PERMV(th1, L[3].h1, L[3].h0, Cd[3][3]);
    M16(acc[1][6], W0[1], X[6]);
    PERMS(W1[3][0], th0, tl0, selA);
    M16(acc[2][6], W0[2], X[6]);
    PERMS(W1[3][1], th0, tl0, selB);
    M16(acc[0][7], W0[0], X[7]);
    PERMS(W1[3][2], th1, tl1, selA);
    M16(acc[1][7], W0[1], X[7]);
    PERMS(W1[3][3], th1, tl1, selB);
    M16(acc[2][7], W0[2], X[7]);
  }
  {
    const unsigned base = ldsbase + row * OSTRIDE + pr * 224 + (q >> 1) * 16;
    const int odd = q & 1;
    const unsigned o01A = base + h * 128 + odd * 32 + h * 64 * OSTRIDE;
    const unsigned o2mA = base + (odd ? 96 : h * 128 + 64) + h * 64 * OSTRIDE;
    const unsigned o01B = base + h * 128 + odd * 32 + (1 - h) * 64 * OSTRIDE;
    const unsigned o22B = base + h * 128 + 64 + odd * 16 * OSTRIDE + (1 - h) * 64 * OSTRIDE;
#define ST2(A, B, addr, off) do { unsigned ax_, ay_, bx_, by_; \
      asm volatile("v_cvt_pk_bf16_f32 %0, %4, %5\n\tv_cvt_pk_bf16_f32 %1, %6, %7\n\tv_cvt_pk_bf16_f32 %2, %8, %9\n\tv_cvt_pk_bf16_f32 %3, %10, %11\n\t" \
                   "s_nop 1\n\tv_permlane16_swap_b32 %0, %2\n\tv_permlane16_swap_b32 %1, %3" \
                   : "=&v"(ax_), "=&v"(ay_), "=&v"(bx_), "=&v"(by_) \
                   : "v"(A[0]), "v"(A[1]), "v"(A[2]), "v"(A[3]), "v"(B[0]), "v"(B[1]), "v"(B[2]), "v"(B[3])); \
      asm volatile("ds_write_b128 %0, %1 offset:%2" :: "v"(addr), "v"(u32x4{ax_, ay_, bx_, by_}), "i"(off) : "memory"); } while (0)
    LGKM(0); BAR();
    M16(acc[0][0], W1[0], X[0]);
    M16(acc[1][0], W1[1], X[0]);
    M16(acc[2][0], W1[2], X[0]);
    M16(accm[0], W1[3], X[0]);
    M16(acc[0][1], W1[0], X[1]);
    M16(acc[1][1], W1[1], X[1]);
    ST2(acc[0][0], acc[1][0], o01A, 0 * 16 * OSTRIDE);
    M16(acc[2][1], W1[2], X[1]);
    M16(accm[1], W1[3], X[1]);
    ST2(acc[2][0], accm[0], o2mA, 0 * 16 * OSTRIDE);
    M16(acc[0][2], W1[0], X[2]);
    M16(acc[1][2], W1[1], X[2]);
    ST2(acc[0][1], acc[1][1], o01A, 1 * 16 * OSTRIDE);
    M16(acc[2][2], W1[2], X[2]);
    M16(accm[2], W1[3], X[2]);
    ST2(acc[2][1], accm[1], o2mA, 1 * 16 * OSTRIDE);
    M16(acc[0][3], W1[0], X[3]);
    M16(acc[1][3], W1[1], X[3]);
    ST2(acc[0][2], acc[1][2], o01A, 2 * 16 * OSTRIDE);
    M16(acc[2][3], W1[2], X[3]);
    M16(accm[3], W1[3], X[3]);
    ST2(acc[2][2], accm[2], o2mA, 2 * 16 * OSTRIDE);
    M16(acc[0][4], W1[0], Y[0]);
    M16(acc[1][4], W1[1], Y[0]);
    ST2(acc[0][3], acc[1][3], o01A, 3 * 16 * OSTRIDE);
    M16(acc[2][4], W1[2], Y[0]);
    M16(acc[0][5], W1[0], Y[1]);
    ST2(acc[2][3], accm[3], o2mA, 3 * 16 * OSTRIDE);
    M16(acc[1][5], W1[1], Y[1]);
    M16(acc[2][5], W1[2], Y[1]);
    ST2(acc[0][4], acc[1][4], o01B, 0 * 16 * OSTRIDE);
    M16(acc[0][6], W1[0], Y[2]);
    M16(acc[1][6], W1[1], Y[2]);
    ST2(acc[0][5], acc[1][5], o01B, 1 * 16 * OSTRIDE);
    M16(acc[2][6], W1[2], Y[2]);
    M16(acc[0][7], W1[0], Y[3]);
    ST2(acc[2][4], acc[2][5], o22B, 0 * 16 * OSTRIDE);
    M16(acc[1][7], W1[1], Y[3]);
    M16(acc[2][7], W1[2], Y[3]);
    ST2(acc[0][6], acc[1][6], o01B, 2 * 16 * OSTRIDE);
    asm volatile("s_nop 7\n\ts_nop 7" ::: "memory");
    ST2(acc[0][7], acc[1][7], o01B, 3 * 16 * OSTRIDE);
    ST2(acc[2][6], acc[2][7], o22B, 2 * 16 * OSTRIDE);
  }
}

__device__ __forceinline__ void xloader(unsigned ldsbase, int pw, int lane, const unsigned short* __restrict__ xt) {
  __amdgpu_buffer_rsrc_t srdX = __builtin_amdgcn_make_buffer_rsrc((void*)xt, (short)0, BM * K_ * 2, 0x00020000);
  const int i4 = lane >> 4;
  const int fE = ((i4 & 1) << 1) | (i4 >> 1);
  const unsigned xvE = (lane >> 3) * 2048 + (((lane & 7) ^ fE) << 4);
  const unsigned xvO = (lane >> 3) * 2048 + (((lane & 7) ^ fE ^ 4) << 4);
  auto issue = [&](int t) {
    unsigned so = (unsigned)(pw * 64 * 2048 + t * 128);
    unsigned m0 = ldsbase + XOFF + ((t & 3) << 14) + pw * 8192;
#pragma unroll
    for (int j = 0; j < 8; ++j) DMA(m0 + j * 1024, (j & 1) ? xvO : xvE, srdX, so + j * 16384);
  };
  BAR();
  BAR();
  issue(2); issue(3);
  BAR();
#pragma unroll 1
  for (int t = 0; t < NSTEP - 4; ++t) {
    issue(t + 4);
    VMC(16);
    BAR();
  }
  VMC(8); BAR();
  VMC(0); BAR();
  BAR();
}

__device__ __forceinline__ void publisher(unsigned ldsbase, int lane, int mt, int nt, __amdgpu_buffer_rsrc_t srdQ,
                                          unsigned char* __restrict__ ws) {
  const int pq = mt & 3, ptb = mt >> 2;
  const int lc = lane < 56 ? lane : 55;
  const unsigned qv = lc * 16;
  Unit U, V;
  unit_load(U, srdQ, qv, (4 + ptb) * 64 + pq * 16);
  __amdgpu_buffer_rsrc_t srdC = code_rsrc(ws, nt);
  const unsigned scr = ldsbase + SCROFF;
  auto stores = [&](int round, unsigned sc) {
#pragma unroll
    for (int p = 0; p < 4; ++p) {
      const int col = p < 3 ? p * 64 + lane : 192 + (lane & 31);
      u32x4 o = *(const lds_u32x4*)LDSP(sc + col * 16);
      const unsigned off = ((4 * round + ptb) * 14 + (col >> 4)) * 1024 + (pq * 16 + (col & 15)) * 16;
      __builtin_amdgcn_raw_buffer_store_b128(o, srdC, off, 0, 16);
    }
    asm volatile("" ::: "memory");
  };
  BAR();
  u32x2 tv = *(const lds_u32x2*)LDSP(ldsbase + TAGOFF);
  const unsigned long long tag = (unsigned long long)tv[1] << 32 | tv[0];
  auto flag = [&](int round) {
    if (lane == 0) __hip_atomic_store((gu64*)(ws + WS_FLAGS) + ((nt * 4 + round) * 16 + mt) * FSTRIDE, tag, RLX_AGENT);
    asm volatile("" ::: "memory");
  };
  UNIT_WAIT(0, U);
  unit_pack<0, 4>(U, scr + lc * 64);
  LGKM(0);
  stores(1, scr);
  unit_load(V, srdQ, qv, (8 + ptb) * 64 + pq * 16);
  BAR();
  VMC(16);
  flag(1);
  BAR();
  UNIT_WAIT(1, V);
  SB(); unit_pack<0, 2>(V, scr + 3584 + lc * 64); SB();
  BAR();
  UNIT_WAIT(1, V);
  unit_pack<2, 4>(V, scr + 3584 + lc * 64); SB();
  LGKM(0);
  stores(2, scr + 3584);
  BAR();
  VMC(0);
  flag(2);
  unit_load(U, srdQ, qv, (12 + ptb) * 64 + pq * 16);
  BAR();
  BAR();
  UNIT_WAIT(0, U);
  SB(); unit_pack<0, 2>(U, scr + lc * 64); SB();
  BAR();
  UNIT_WAIT(0, U);
  unit_pack<2, 4>(U, scr + lc * 64); SB();
  LGKM(0);
  stores(3, scr);
  BAR();
  VMC(0);
  flag(3);
#pragma unroll 1
  for (int t = 7; t < NSTEP; ++t) {
    if (t == 9 && blockIdx.x == 0) {
      gu64* f = (gu64*)(ws + WS_FLAGS) + ((lane >> 2) * 64 + 16 + (lane & 3) * 4) * FSTRIDE;
#pragma unroll 1
      for (unsigned spins = 0; spins < SPIN_CAP; ++spins) {
        bool ok = true;
#pragma unroll
        for (int i = 0; i < 4; ++i) ok &= __hip_atomic_load(f + i * FSTRIDE, RLX_AGENT) == tag;
        if (__all(ok)) break;
        __builtin_amdgcn_s_sleep(1);
      }
      if (lane == 0) __hip_atomic_store((gu64*)(ws + WS_E), tag, RLX_AGENT);
    }
    BAR();
  }
}

__device__ __forceinline__ void cloader(unsigned ldsbase, int lane, int mt, int nt, unsigned char* __restrict__ ws) {
  __amdgpu_buffer_rsrc_t srdC = code_rsrc(ws, nt);
  const unsigned cv = lane * 16;
  auto issue = [&](int tile) {
    unsigned so = (unsigned)(tile * CSTAGE);
    unsigned m0 = ldsbase + COFF + (tile & 3) * CSTAGE;
    DMA4_SC1(m0, cv, srdC, so); DMA4_SC1(m0 + 4096, cv, srdC, so + 4096); DMA4_SC1(m0 + 8192, cv, srdC, so + 8192);
    DMA2_SC1(m0 + 12288, cv, srdC, so + 12288);
  };
  BAR();
  u32x2 tv = *(const lds_u32x2*)LDSP(ldsbase + TAGOFF);
  const unsigned long long tag = (unsigned long long)tv[1] << 32 | tv[0];
  if (lane == 0 && mt >= 8) __hip_atomic_store((gu64*)(ws + WS_FLAGS) + (nt * 64 + mt) * FSTRIDE, tag, RLX_AGENT);
  auto pollp = [&](gu64* f) {
#pragma unroll 1
    for (unsigned spins = 0; spins < SPIN_CAP; ++spins) {
      unsigned long long v = __hip_atomic_load(f, RLX_AGENT);
      if (__all(v == tag)) break;
      __builtin_amdgcn_s_sleep(2);
    }
    asm volatile("" ::: "memory");
  };
  BAR();
  u32x2 fv;
  auto prepoll = [&](int round) {
    gu64* f = (gu64*)(ws + WS_FLAGS) + ((nt * 4 + round) * 16 + (lane & 15)) * FSTRIDE;
    asm volatile("global_load_dwordx2 %0, %1, off sc1" : "=v"(fv) : "v"(f) : "memory");
  };
  auto check = [&](int round) {
    asm volatile("s_waitcnt vmcnt(0)" : "+v"(fv) :: "memory");
    if (!__all(fv[0] == (unsigned)tag && fv[1] == (unsigned)(tag >> 32)))
      pollp((gu64*)(ws + WS_FLAGS) + ((nt * 4 + round) * 16 + (lane & 15)) * FSTRIDE);
  };
  pollp((gu64*)(ws + WS_FLAGS) + (nt * 64 + 8 + (lane & 7)) * FSTRIDE);
  issue(2);
  BAR();
  issue(3);
  prepoll(1);
  VMC(15);
  BAR();
#pragma unroll 1
  for (int r = 1; r < 4; ++r) {
    check(r);
    issue(4 * r);
    BAR();
    issue(4 * r + 1);
    VMC(14);
    BAR();
    issue(4 * r + 2);
    VMC(14);
    BAR();
    issue(4 * r + 3);
    if (r < 3) {
      prepoll(r + 1);
      VMC(15);
      BAR();
    }
  }
  VMC(14); BAR();
  VMC(0); BAR();
  BAR();
}

__global__ __launch_bounds__(512, 2) void squeezellm_gemm_kernel(const unsigned short* __restrict__ x, const int* __restrict__ qw,
                                                                 const unsigned short* __restrict__ lut, unsigned short* __restrict__ out,
                                                                 unsigned char* __restrict__ ws) {
  __shared__ __attribute__((aligned(16))) char lds[LDS_BYTES];
  const unsigned ldsbase = (unsigned)(uintptr_t)(__attribute__((address_space(3))) char*)lds;
  const int tid = threadIdx.x, lane = tid & 63;
  const int wid = __builtin_amdgcn_readfirstlane(tid >> 6);
  const int bid = blockIdx.x, xcd = bid & 7, idx = bid >> 3;
  const int mt = (xcd & 3) * 4 + (idx & 3), nt = (xcd >> 2) * 8 + (idx >> 2);
  __amdgpu_buffer_rsrc_t srdQ = __builtin_amdgcn_make_buffer_rsrc((void*)(qw + nt * BN), (short)0, (K_ * N_ - nt * BN) * 4, 0x00020000);
  if (__builtin_expect(wid < 4, 1)) {
    consumer(ldsbase, wid >> 1, wid & 1, lane, mt, nt, srdQ, lut + nt * BN, ws, x + (long)(mt * BM) * K_);
  } else if (wid < 6) {
    xloader(ldsbase, wid - 4, lane, x + (long)(mt * BM) * K_);
  } else if (wid == 6) {
    cloader(ldsbase, lane, mt, nt, ws);
  } else {
    publisher(ldsbase, lane, mt, nt, srdQ, ws);
  }
  LGKM(0);
  BAR();
  unsigned short* ot = out + (long)(mt * BM) * N_ + nt * BN;
  __amdgpu_buffer_rsrc_t srdO = __builtin_amdgcn_make_buffer_rsrc((void*)ot, (short)0, (127 * N_ + BN) * 2, 0x00020000);
#pragma unroll
  for (int i = 0; i < 7; ++i) {
    int ch = i * 512 + tid;
    int row = ch / 28, cc = ch - row * 28;
    u32x4 v = *(const lds_u32x4*)LDSP(ldsbase + row * OSTRIDE + cc * 16);
    __builtin_amdgcn_raw_buffer_store_b128(v, srdO, (unsigned)(row * N_ + cc * 8) * 2, 0, 17  );
  }
}

extern "C" void kernel_launch(void* const* d_in, const int* in_sizes, int n_in, void* d_out, int out_size, void* d_ws,
                              size_t ws_size, hipStream_t stream) {
  const unsigned short* x = (const unsigned short*)d_in[0];
  const int* qw = (const int*)d_in[1];
  const unsigned short* lut = (const unsigned short*)d_in[2];
  squeezellm_gemm_kernel<<<256, 512, 0, stream>>>(x, qw, lut, (unsigned short*)d_out, (unsigned char*)d_ws);
}
```
